```python
import torch

def _softmax(x, axis=-1):
    return torch.softmax(x, dim=axis)
def _gelu(x):
    return torch.nn.functional.gelu(x, approximate="tanh")
def _silu(x):
    return torch.nn.functional.silu(x)
def _sigmoid(x):
    return torch.sigmoid(x)
def _layernorm(x, axis=-1, eps=1e-5):
    m = x.mean(dim=axis, keepdim=True)
    v = x.var(dim=axis, keepdim=True, unbiased=False)
    return (x - m) / torch.sqrt(v + eps)
def _scatter_add(tgt, idx, src, axis=0):
    return tgt.clone().index_add_(axis, idx, src)
def _segment_sum(src, seg, n):
    out = torch.zeros((n,) + src.shape[1:], dtype=src.dtype)
    return out.index_add_(0, seg, src)
def _pad(x, pw):
    flat = tuple(v for pair in reversed(pw) for v in pair)
    return torch.nn.functional.pad(x, flat)

kernel_name = 'parallel_sum__softmax_full_mha__moe_arch_mod__prompt'

def setup_inputs():

    _w = (lambda a: (lambda t: t.to(torch.bfloat16) if t.is_floating_point() else t)(torch.as_tensor(a)))
    def _A_setup():
        B, S, D = 1, 2048, 640
        return {
            "x": _w((lambda *s: torch.randn(*s, dtype=torch.bfloat16) * 0.05)(B, S, D)),
            "wqkv": _w((lambda *s: torch.randn(*s, dtype=torch.bfloat16) * 0.2)(D, 3 * D)),
            "wo": _w((lambda *s: torch.randn(*s, dtype=torch.bfloat16) * 0.2)(D, D)),
        }
    _A_in = _A_setup()

    def _B_setup():
        B, S, D, F, C = 1, 2048, 640, 1728, 2
        return {
            "x": _w((lambda *s: torch.randn(*s, dtype=torch.bfloat16) * 0.05)(B, S, D)),
            "w_r": _w((lambda *s: torch.randn(*s, dtype=torch.bfloat16) * 0.8)(D)),
            "w_gate": _w((lambda *s: torch.randn(*s, dtype=torch.bfloat16) * 0.2)(D, F)),
            "w_up": _w((lambda *s: torch.randn(*s, dtype=torch.bfloat16) * 0.2)(D, F)),
            "w_down": _w((lambda *s: torch.randn(*s, dtype=torch.bfloat16) * 0.2)(F, D)),
        }
    _B_in = _B_setup()

    return {
        "A_x": _A_in["x"],
        "A_wqkv": _A_in["wqkv"],
        "A_wo": _A_in["wo"],
        "B_x": _B_in["x"],
        "B_w_r": _B_in["w_r"],
        "B_w_gate": _B_in["w_gate"],
        "B_w_up": _B_in["w_up"],
        "B_w_down": _B_in["w_down"],
    }

def reference(A_x, A_wqkv, A_wo, B_x, B_w_r, B_w_gate, B_w_up, B_w_down):
    def _A_ref(x, wqkv, wo):
        B, S, D = x.shape
        H = 32; Dh = D // H
        qkv = (lambda x, *d: x.permute(*d))((x @ wqkv).reshape(B, S, 3, H, Dh), 2, 0, 3, 1, 4)
        q, k, v = qkv[0], qkv[1], qkv[2]
        scale = Dh ** -0.5
        scores = torch.einsum("bhqd,bhkd->bhqk", q, k) * scale
        p = _softmax(scores, axis=-1)
        o = torch.einsum("bhqk,bhkd->bhqd", p, v)
        return (lambda x, *d: x.permute(*d))(o, 0, 2, 1, 3).reshape(B, S, D) @ wo

    def _B_ref(x, w_r, w_gate, w_up, w_down):
        B, S, D = x.shape
        C = 2
        r = x @ w_r
        idx = torch.argsort(-r, axis=-1)[:, :C]
        idx_e = (lambda x, n, axis: torch.repeat_interleave(x, n, dim=axis))(idx.reshape(B, C, 1), D, axis=2)
        x_sel = (lambda x, i, axis: torch.take_along_dim(x, i, dim=axis))(x, idx_e, axis=1)
        g = _silu(x_sel @ w_gate)
        u = x_sel @ w_up
        y = (g * u) @ w_down
        r_sel = _sigmoid((lambda x, i, axis: torch.take_along_dim(x, i, dim=axis))(r, idx, axis=1))
        y = y * r_sel.reshape(B, C, 1)
        b_off = (torch.arange(B) * S).reshape(B, 1)
        flat_idx = (idx + b_off).reshape(B * C)
        zero = (x * 0.0).reshape(B * S, D)
        upd = _scatter_add(zero, flat_idx, y.reshape(B * C, D), axis=0)
        return x + upd.reshape(B, S, D)

    x = A_x
    xn = _layernorm(x, axis=-1)
    return x + _A_ref(xn, A_wqkv, A_wo) + _B_ref(xn, B_w_r, B_w_gate, B_w_up, B_w_down)
```

```cpp
#include <hip/hip_runtime.h>
#include <hip/hip_bf16.h>

typedef __bf16 bf16;
typedef __bf16 bf16x2 __attribute__((ext_vector_type(2)));
typedef __bf16 bf16x4 __attribute__((ext_vector_type(4)));
typedef __bf16 bf16x8 __attribute__((ext_vector_type(8)));
typedef short s16x4 __attribute__((ext_vector_type(4)));
typedef float f32x4 __attribute__((ext_vector_type(4)));
typedef float f32x16 __attribute__((ext_vector_type(16)));
typedef unsigned u32x2 __attribute__((ext_vector_type(2)));
typedef unsigned u32x4 __attribute__((ext_vector_type(4)));
#define LDS3 __attribute__((address_space(3)))

constexpr int S_ = 2048, D_ = 640, H_ = 32, F_ = 1728;
constexpr int QT_STRIDE = 1280;
constexpr int KV_STRIDE = 2624;
constexpr size_t WS_Q = 0, WS_KV = 0x300000, WS_O = 0x900000, WS_XN = 0xC00000, WS_R = 0xF00000,
                 WS_SEL = 0xF04000, WS_YP = 0xF10000;
constexpr int N_MOE = 54, N_MOE_PAD = 56;
constexpr float QSCALE = 0.22360679774997896f * 1.4426950408889634f;

__device__ __forceinline__ float bf2f(bf16 v) { return (float)v; }
__device__ __forceinline__ float rbf(float v) { return (float)(bf16)v; }
__device__ __forceinline__ unsigned pack2(float a, float b) {
  bf16x2 p; p[0] = (bf16)a; p[1] = (bf16)b; return __builtin_bit_cast(unsigned, p);
}
__device__ __forceinline__ float lo16(unsigned u) { return __builtin_bit_cast(float, u << 16); }
__device__ __forceinline__ float hi16(unsigned u) { return __builtin_bit_cast(float, u & 0xffff0000u); }
__device__ __forceinline__ void glds16(const void* g, void* l) {
  __builtin_amdgcn_global_load_lds((const unsigned*)g, (LDS3 unsigned*)l, 16, 0, 0);
}
__device__ __forceinline__ s16x4 trread(const char* l) {
  return __builtin_amdgcn_ds_read_tr16_b64_v4i16((LDS3 s16x4*)l);
}
__device__ __forceinline__ bf16x8 comb(s16x4 a, s16x4 b) {
  typedef short s16x8 __attribute__((ext_vector_type(8)));
  s16x8 r = __builtin_shufflevector(a, b, 0, 1, 2, 3, 4, 5, 6, 7);
  return __builtin_bit_cast(bf16x8, r);
}
__device__ __forceinline__ float dot2(unsigned a, unsigned b, float c) {
  return __builtin_amdgcn_fdot2_f32_bf16(__builtin_bit_cast(bf16x2, a), __builtin_bit_cast(bf16x2, b), c, false);
}
#define WAITVM(n) asm volatile("s_waitcnt vmcnt(" #n ")" ::: "memory")
#define BARRIER() do { asm volatile("s_waitcnt lgkmcnt(0)" ::: "memory"); __builtin_amdgcn_s_barrier(); } while (0)

template <bool SWAP>
__device__ __forceinline__ void k1_gemm(const bf16* __restrict__ x, const bf16* __restrict__ w, char* __restrict__ ws,
                                        char* smem, int tm, int tn, const u32x4 (&xf)[40]) {
  const int tid = threadIdx.x, wave = __builtin_amdgcn_readfirstlane(tid >> 6), lane = tid & 63, r = lane & 31, h = lane >> 5;
  const int g = lane >> 4, q = (lane & 15) >> 2, p = lane & 3;
  int abase[4][2];
#pragma unroll
  for (int j = 0; j < 4; ++j)
#pragma unroll
    for (int rd = 0; rd < 2; ++rd) {
      int cb = (j + wave) & 3;
      int ch = cb * 4 + (g & 1) * 2 + (p >> 1);
      int swz = (q << 2) | ((2 * h + rd) & 3);
      abase[j][rd] = 256 * (8 * h + 4 * rd + q) + 16 * (ch ^ swz) + 8 * (p & 1);
    }
  f32x16 acc[4];
#pragma unroll
  for (int j = 0; j < 4; ++j)
#pragma unroll
    for (int i = 0; i < 16; ++i) acc[j][i] = 0.f;
  float sm = 0.f, sq = 0.f, cs = 0.f;
  const unsigned ONES = 0x3f803f80u;
  const int lrow = lane >> 4;
  auto issue = [&](int c, int slot) {
#pragma unroll
    for (int i = 0; i < 4; ++i) {
      int P = wave * 4 + i;
      int row = 4 * P + lrow;
      int ch = (lane & 15) ^ ((lrow << 2) | (P & 3));
      glds16(w + (size_t)(64 * c + row) * 1920 + tn * 128 + ch * 8, smem + slot * 16384 + P * 1024);
    }
  };
#pragma unroll
  for (int c = 0; c < 10; ++c) {
    switch (c) {
      case 0: WAITVM(28); break; case 1: WAITVM(24); break; case 2: WAITVM(24); break; case 3: WAITVM(24); break;
      case 4: WAITVM(20); break; case 5: WAITVM(16); break; case 6: WAITVM(12); break; case 7: WAITVM(8); break;
      case 8: WAITVM(4); break; default: WAITVM(0); break;
    }
    BARRIER();
    if (c == 1 || c == 2) issue(c + 7, c - 1);
    const char* sl = smem + (c & 7) * 16384;
#pragma unroll
    for (int ks = 0; ks < 4; ++ks) {
      const u32x4 xa = xf[c * 4 + ks];
      const bf16x8 xb = __builtin_bit_cast(bf16x8, xa);
#pragma unroll
      for (int j = 0; j < 4; ++j) {
        s16x4 w0 = trread(sl + ks * 4096 + abase[j][0]);
        s16x4 w1 = trread(sl + ks * 4096 + abase[j][1]);
        bf16x8 wf = comb(w0, w1);
        if (SWAP) acc[j] = __builtin_amdgcn_mfma_f32_32x32x16_bf16(wf, xb, acc[j], 0, 0, 0);
        else      acc[j] = __builtin_amdgcn_mfma_f32_32x32x16_bf16(xb, wf, acc[j], 0, 0, 0);
        if (j == 0) {
          u32x4 wu = __builtin_bit_cast(u32x4, wf);
          cs = dot2(wu[0], ONES, cs); cs = dot2(wu[1], ONES, cs); cs = dot2(wu[2], ONES, cs); cs = dot2(wu[3], ONES, cs);
        }
      }
#pragma unroll
      for (int i = 0; i < 4; ++i) { sm = dot2(xa[i], ONES, sm); sq = dot2(xa[i], xa[i], sq); }
    }
  }
  sm += __shfl_xor(sm, 32); sq += __shfl_xor(sq, 32); cs += __shfl_xor(cs, 32);
  float* s_mean = (float*)(smem + 131072);
  float* s_rstd = s_mean + 128;
  float* s_cs = s_rstd + 128;
  const float mean = sm * (1.f / 640.f);
  const float var = fmaxf(sq * (1.f / 640.f) - mean * mean, 0.f);
  const float meanb = rbf(mean);
  const float rstd = 1.f / rbf(sqrtf(rbf(rbf(var) + 1e-5f)));
  if (h == 0) { s_mean[wave * 32 + r] = meanb; s_rstd[wave * 32 + r] = rstd; s_cs[wave * 32 + r] = cs; }
  BARRIER();
  const int tile = tm * 4 + wave;
  if (SWAP) {
    const int part = tn / 5;
    const float sc = part == 0 ? rstd * QSCALE : rstd;
    char* base = ws + (part == 0 ? WS_Q : WS_KV);
    const int stride = part == 0 ? QT_STRIDE : KV_STRIDE;
#pragma unroll
    for (int j = 0; j < 4; ++j) {
      const int cb = (j + wave) & 3;
#pragma unroll
      for (int g4 = 0; g4 < 4; ++g4) {
        const int nl = cb * 32 + 8 * g4 + 4 * h;
        const int nn = (tn - part * 5) * 128 + nl;
        const int head = nn / 20, d0 = nn - head * 20;
        const f32x4 c4 = *(const f32x4*)(s_cs + nl);
        float v0 = sc * (acc[j][4 * g4 + 0] - meanb * c4[0]), v1 = sc * (acc[j][4 * g4 + 1] - meanb * c4[1]);
        float v2 = sc * (acc[j][4 * g4 + 2] - meanb * c4[2]), v3 = sc * (acc[j][4 * g4 + 3] - meanb * c4[3]);
        u32x2 o; o[0] = pack2(v0, v1); o[1] = pack2(v2, v3);
        const int off = d0 < 16 ? (d0 >> 3) * 512 + r * 16 + (d0 & 4) * 2 : 1024 + r * 8;
        *(u32x2*)(base + (size_t)(head * 64 + tile) * stride + off) = o;
      }
    }
  } else {
#pragma unroll
    for (int j = 0; j < 4; ++j) {
      const int cb = (j + wave) & 3;
      const int nl = cb * 32 + r;
      const int nn = (tn - 10) * 128 + nl;
      const int head = nn / 20, d = nn - head * 20;
      const float c1 = s_cs[nl];
      char* dst = ws + WS_KV + (size_t)(head * 64 + tile) * KV_STRIDE + 1280 + h * 336 + d * 16;
#pragma unroll
      for (int s = 0; s < 2; ++s) {
        float v[8];
#pragma unroll
        for (int hh = 0; hh < 2; ++hh) {
          const int t0 = wave * 32 + 16 * s + 8 * hh + 4 * h;
          const f32x4 m4 = *(const f32x4*)(s_mean + t0), r4 = *(const f32x4*)(s_rstd + t0);
#pragma unroll
          for (int i = 0; i < 4; ++i) v[hh * 4 + i] = r4[i] * (acc[j][8 * s + 4 * hh + i] - m4[i] * c1);
        }
        u32x4 o; o[0] = pack2(v[0], v[1]); o[1] = pack2(v[2], v[3]); o[2] = pack2(v[4], v[5]); o[3] = pack2(v[6], v[7]);
        *(u32x4*)(dst + s * 672) = o;
        if (d == 19) { u32x4 one = {ONES, ONES, ONES, ONES}; *(u32x4*)(dst + s * 672 + 16) = one; }
      }
    }
  }
}

__global__ __launch_bounds__(256, 1) void k1_ln_qkv(const bf16* __restrict__ x, const bf16* __restrict__ w,
                                                    const bf16* __restrict__ wr, char* __restrict__ ws) {
  extern __shared__ __attribute__((aligned(16))) char smem[];
  const int b = blockIdx.x, tid = threadIdx.x, wave = __builtin_amdgcn_readfirstlane(tid >> 6), lane = tid & 63, r = lane & 31, h = lane >> 5;
  const bool gemm = b < 240;
  const int i = (b & 7) * 30 + (b >> 3);
  const int tm = i / 15, tn = i - tm * 15;
  const int lrow = b * 8 + wave * 2 + h;
  u32x2 xr[5], wv[5];
#pragma unroll
  for (int k = 0; k < 5; ++k) {
    xr[k] = *(const u32x2*)(x + (size_t)lrow * D_ + r * 20 + k * 4);
    wv[k] = *(const u32x2*)(wr + r * 20 + k * 4);
  }
  u32x4 xf[40];
  if (gemm) {
    const bf16* xp = x + (size_t)(tm * 128 + wave * 32 + r) * D_ + 8 * h;
#pragma unroll
    for (int s = 0; s < 40; ++s) xf[s] = *(const u32x4*)(xp + 16 * s);
    const int lr = lane >> 4;
#pragma unroll
    for (int c = 0; c < 8; ++c)
#pragma unroll
      for (int k = 0; k < 4; ++k) {
        int P = wave * 4 + k;
        int row = 4 * P + lr;
        int ch = (lane & 15) ^ ((lr << 2) | (P & 3));
        glds16(w + (size_t)(64 * c + row) * 1920 + tn * 128 + ch * 8, smem + c * 16384 + P * 1024);
      }
  }
  {
    float v[20];
#pragma unroll
    for (int k = 0; k < 5; ++k) {
      v[4 * k] = lo16(xr[k][0]); v[4 * k + 1] = hi16(xr[k][0]); v[4 * k + 2] = lo16(xr[k][1]); v[4 * k + 3] = hi16(xr[k][1]);
    }
    float s = 0.f;
#pragma unroll
    for (int k = 0; k < 20; ++k) s += v[k];
#pragma unroll
    for (int o = 16; o >= 1; o >>= 1) s += __shfl_xor(s, o);
    const float mean = s * (1.f / 640.f);
    float q = 0.f;
#pragma unroll
    for (int k = 0; k < 20; ++k) { float d = v[k] - mean; q += d * d; }
#pragma unroll
    for (int o = 16; o >= 1; o >>= 1) q += __shfl_xor(q, o);
    const float meanb = rbf(mean);
    const float den = rbf(sqrtf(rbf(rbf(q * (1.f / 640.f)) + 1e-5f)));
    float rr = 0.f;
    u32x2 o2[5];
#pragma unroll
    for (int k = 0; k < 5; ++k) {
      float n0 = rbf(rbf(v[4 * k] - meanb) / den), n1 = rbf(rbf(v[4 * k + 1] - meanb) / den);
      float n2 = rbf(rbf(v[4 * k + 2] - meanb) / den), n3 = rbf(rbf(v[4 * k + 3] - meanb) / den);
      rr += n0 * lo16(wv[k][0]) + n1 * hi16(wv[k][0]) + n2 * lo16(wv[k][1]) + n3 * hi16(wv[k][1]);
      o2[k][0] = pack2(n0, n1); o2[k][1] = pack2(n2, n3);
    }
#pragma unroll
    for (int o = 16; o >= 1; o >>= 1) rr += __shfl_xor(rr, o);
    bf16* xn = (bf16*)(ws + WS_XN) + (size_t)lrow * D_ + r * 20;
#pragma unroll
    for (int k = 0; k < 5; ++k) *(u32x2*)(xn + 4 * k) = o2[k];
    if (r == 0) ((float*)(ws + WS_R))[lrow] = rbf(rr);
  }
  if (!gemm) return;
  if (tn < 10) k1_gemm<true>(x, w, ws, smem, tm, tn, xf);
  else k1_gemm<false>(x, w, ws, smem, tm, tn, xf);
}

constexpr int SLOT = 11264;
constexpr float RECENTER = 1.099511627776e12f;
constexpr float OVERFLOW_GUARD = 1.2676506e30f;

__device__ __forceinline__ bf16x8 packP(const f32x16& s, int o) {
  bf16x8 p;
#pragma unroll
  for (int i = 0; i < 8; ++i) p[i] = (bf16)__builtin_amdgcn_exp2f(s[o + i]);
  return p;
}

__device__ void moe_block(int mb, const bf16* __restrict__ wg, const bf16* __restrict__ wu, const bf16* __restrict__ wd,
                          char* __restrict__ ws, char* smem) {
  const int tid = threadIdx.x, wave = tid >> 6, lane = tid & 63;
  const float* rv = (const float*)(ws + WS_R);
  unsigned long long* s_key = (unsigned long long*)smem;
  float* s_xn = (float*)(smem + 256);
  float* s_red = (float*)(smem + 256 + 5120);
  float* s_h = (float*)(smem + 256 + 5120 + 30720);
  unsigned long long key[4];
#pragma unroll
  for (int i = 0; i < 4; ++i) {
    int idx = tid + 512 * i;
    unsigned u = __builtin_bit_cast(unsigned, rv[idx]);
    u = (u & 0x80000000u) ? ~u : (u | 0x80000000u);
    key[i] = ((unsigned long long)u << 32) | (unsigned)(0xFFFFFFFFu - idx);
  }
  unsigned long long top[2];
#pragma unroll
  for (int rnd = 0; rnd < 2; ++rnd) {
    unsigned long long m = 0;
#pragma unroll
    for (int i = 0; i < 4; ++i) { unsigned long long k = (rnd == 1 && key[i] == top[0]) ? 0ull : key[i]; m = k > m ? k : m; }
#pragma unroll
    for (int o = 32; o >= 1; o >>= 1) { unsigned long long t = __shfl_xor(m, o); m = t > m ? t : m; }
    if (lane == 0) s_key[rnd * 8 + wave] = m;
    __syncthreads();
    m = 0;
#pragma unroll
    for (int i = 0; i < 8; ++i) { unsigned long long t = s_key[rnd * 8 + i]; m = t > m ? t : m; }
    top[rnd] = m;
  }
  const int i0 = (int)(0xFFFFFFFFu - (unsigned)top[0]), i1 = (int)(0xFFFFFFFFu - (unsigned)top[1]);
  if (mb == 0 && tid < 2) {
    int id = tid == 0 ? i0 : i1;
    ((int*)(ws + WS_SEL))[tid] = id;
    ((float*)(ws + WS_SEL))[2 + tid] = rbf(1.f / (1.f + __expf(-rv[id])));
  }
  const bf16* xn = (const bf16*)(ws + WS_XN);
  for (int i = tid; i < 1280; i += 512) { int t = i >= 640; int k = i - t * 640; s_xn[i] = bf2f(xn[(size_t)(t ? i1 : i0) * D_ + k]); }
  const int f0 = mb * 32, quad = tid & 3, mat = (tid >> 2) & 1, rg = tid >> 3;
  const bf16* wp = (mat ? wu : wg) + f0 + quad * 8;
  __syncthreads();
  float a[2][8];
#pragma unroll
  for (int j = 0; j < 8; ++j) { a[0][j] = 0.f; a[1][j] = 0.f; }
#pragma unroll 1
  for (int i0b = 0; i0b < 10; i0b += 5) {
    u32x4 wl[5];
#pragma unroll
    for (int i = 0; i < 5; ++i) wl[i] = *(const u32x4*)(wp + (size_t)(rg + 64 * (i0b + i)) * F_);
#pragma unroll
    for (int i = 0; i < 5; ++i) {
      float x0 = s_xn[rg + 64 * (i0b + i)], x1 = s_xn[640 + rg + 64 * (i0b + i)];
#pragma unroll
      for (int j = 0; j < 4; ++j) {
        float wl0 = lo16(wl[i][j]), wh0 = hi16(wl[i][j]);
        a[0][2 * j] += x0 * wl0; a[0][2 * j + 1] += x0 * wh0; a[1][2 * j] += x1 * wl0; a[1][2 * j + 1] += x1 * wh0;
      }
    }
  }
  const int c = tid % 80, rgd = tid / 80;
  u32x4 dl[6];
#pragma unroll
  for (int i = 0; i < 6; ++i) {
    int f = rgd + 6 * i;
    dl[i] = (rgd < 6 && f < 32) ? *(const u32x4*)(wd + (size_t)(f0 + f) * D_ + c * 8) : u32x4{0, 0, 0, 0};
  }
#pragma unroll
  for (int t = 0; t < 2; ++t)
#pragma unroll
    for (int j = 0; j < 8; ++j) {
      float v = a[t][j];
      v += __shfl_xor(v, 8); v += __shfl_xor(v, 16); v += __shfl_xor(v, 32);
      if (lane < 8) s_red[(wave * 8 + lane) * 16 + t * 8 + j] = v;
    }
  __syncthreads();
  if (tid < 64) {
    int t = tid >> 5, col = tid & 31, qd = col >> 3, j = col & 7;
    float gs = 0.f, us = 0.f;
#pragma unroll
    for (int w = 0; w < 8; ++w) { gs += s_red[(w * 8 + qd) * 16 + t * 8 + j]; us += s_red[(w * 8 + 4 + qd) * 16 + t * 8 + j]; }
    gs = rbf(gs); us = rbf(us);
    float sl = rbf(gs / (1.f + __expf(-gs)));
    s_h[t * 32 + col] = rbf(sl * us);
  }
  __syncthreads();
#pragma unroll
  for (int j = 0; j < 8; ++j) { a[0][j] = 0.f; a[1][j] = 0.f; }
#pragma unroll
  for (int i = 0; i < 6; ++i) {
    int f = rgd + 6 * i;
    float h0 = (rgd < 6 && f < 32) ? s_h[f] : 0.f, h1 = (rgd < 6 && f < 32) ? s_h[32 + f] : 0.f;
#pragma unroll
    for (int j = 0; j < 4; ++j) {
      float wl0 = lo16(dl[i][j]), wh0 = hi16(dl[i][j]);
      a[0][2 * j] += h0 * wl0; a[0][2 * j + 1] += h0 * wh0; a[1][2 * j] += h1 * wl0; a[1][2 * j + 1] += h1 * wh0;
    }
  }
  if (rgd < 6) {
#pragma unroll
    for (int t = 0; t < 2; ++t)
#pragma unroll
      for (int j = 0; j < 8; ++j) s_red[(rgd * 2 + t) * 640 + c * 8 + j] = a[t][j];
  }
  __syncthreads();
  float* yp = (float*)(ws + WS_YP) + (size_t)mb * 1280;
  for (int i = tid; i < 1280; i += 512) {
    float v = 0.f;
#pragma unroll
    for (int g = 0; g < 6; ++g) v += s_red[g * 1280 + i];
    yp[i] = v;
  }
}

__global__ __launch_bounds__(512, 4) void k2_attn(const bf16* __restrict__ wg, const bf16* __restrict__ wu,
                                                  const bf16* __restrict__ wd, char* __restrict__ ws) {
  extern __shared__ __attribute__((aligned(16))) char smem[];
  if (blockIdx.x < N_MOE_PAD) {
    if (blockIdx.x < N_MOE) moe_block(blockIdx.x, wg, wu, wd, ws, smem);
    return;
  }
  const int a = blockIdx.x - N_MOE_PAD;
  const int head = (a & 7) * 4 + ((a >> 3) & 3), qb = a >> 5;
  const int tid = threadIdx.x, wave = __builtin_amdgcn_readfirstlane(tid >> 6), lane = tid & 63, r = lane & 31, h = lane >> 5;
  const int qg = wave & 3, kh = wave >> 2;
  const char* kvg = ws + WS_KV + (size_t)head * 64 * KV_STRIDE;
  auto issue = [&](int c, int slot) {
    const char* src = kvg + (size_t)c * (4 * KV_STRIDE) + lane * 16;
    glds16(src + wave * 1024, smem + slot * SLOT + wave * 1024);
    if (wave < 3) glds16(src + (8 + wave) * 1024, smem + slot * SLOT + (8 + wave) * 1024);
  };
  issue(0, 0);
  issue(1, 1);
  const char* qp = ws + WS_Q + (size_t)(head * 64 + qb * 4 + qg) * QT_STRIDE;
  const bf16x8 q0 = *(const bf16x8*)(qp + lane * 16);
  u32x2 q1u = *(const u32x2*)(qp + 1024 + r * 8);
  if (h) { q1u[0] = 0; q1u[1] = 0; }
  const bf16x4 q1 = __builtin_bit_cast(bf16x4, q1u);
  const int aK0 = kh * 2 * KV_STRIDE + lane * 16;
  const int aK1 = kh * 2 * KV_STRIDE + 1024 + r * 8;
  const int aV = kh * 2 * KV_STRIDE + 1280 + h * 336 + (r < 20 ? r : 20) * 16;
  f32x16 O, negm;
#pragma unroll
  for (int i = 0; i < 16; ++i) O[i] = 0.f;
  bool bad = false;
  int slot = 0;
  for (int c = 0; c < 16; ++c) {
    if (c < 15) { if (wave < 3) WAITVM(2); else WAITVM(1); } else WAITVM(0);
    BARRIER();
    if (c + 2 < 16) issue(c + 2, slot == 0 ? 2 : slot - 1);
    const char* sl = smem + slot * SLOT;
    if (c == 0) {
      f32x16 z;
#pragma unroll
      for (int i = 0; i < 16; ++i) z[i] = 0.f;
      float mx = -3.0e38f;
#pragma unroll
      for (int t = 0; t < 2; ++t) {
        const bf16x8 k0 = *(const bf16x8*)(sl + t * KV_STRIDE + aK0);
        const bf16x4 k1 = *(const bf16x4*)(sl + t * KV_STRIDE + aK1);
        f32x16 s = __builtin_amdgcn_mfma_f32_32x32x16_bf16(k0, q0, z, 0, 0, 0);
        s = __builtin_amdgcn_mfma_f32_32x32x8bf16_1k(k1, q1, s, 0, 0, 0);
#pragma unroll
        for (int i = 0; i < 16; ++i) mx = fmaxf(mx, s[i]);
      }
      mx = fmaxf(mx, __shfl_xor(mx, 32));
#pragma unroll
      for (int i = 0; i < 16; ++i) negm[i] = -mx;
    } else if (__builtin_amdgcn_ballot_w64(!(O[8] <= RECENTER)) != 0) {
      float l = __shfl(O[8], lane | 32);
      if (!(l < OVERFLOW_GUARD)) bad = true;
      int e = __builtin_amdgcn_frexp_expf(l);
      float nm = negm[0] - (float)e;
#pragma unroll
      for (int i = 0; i < 16; ++i) { O[i] = __builtin_amdgcn_ldexpf(O[i], -e); negm[i] = nm; }
    }
#pragma unroll
    for (int t = 0; t < 2; ++t) {
      const bf16x8 k0 = *(const bf16x8*)(sl + t * KV_STRIDE + aK0);
      const bf16x4 k1 = *(const bf16x4*)(sl + t * KV_STRIDE + aK1);
      const bf16x8 v0 = *(const bf16x8*)(sl + t * KV_STRIDE + aV);
      const bf16x8 v1 = *(const bf16x8*)(sl + t * KV_STRIDE + aV + 672);
      f32x16 s = __builtin_amdgcn_mfma_f32_32x32x16_bf16(k0, q0, negm, 0, 0, 0);
      s = __builtin_amdgcn_mfma_f32_32x32x8bf16_1k(k1, q1, s, 0, 0, 0);
      const bf16x8 p0 = packP(s, 0), p1 = packP(s, 8);
      O = __builtin_amdgcn_mfma_f32_32x32x16_bf16(v0, p0, O, 0, 0, 0);
      O = __builtin_amdgcn_mfma_f32_32x32x16_bf16(v1, p1, O, 0, 0, 0);
    }
    slot = slot == 2 ? 0 : slot + 1;
  }
  {
    float l = __shfl(O[8], lane | 32);
    if (!(l < OVERFLOW_GUARD)) bad = true;
  }
  float m = -negm[0];
  if (__builtin_amdgcn_ballot_w64(bad) != 0) {
    m = -3.0e38f;
#pragma unroll
    for (int i = 0; i < 16; ++i) O[i] = 0.f;
    f32x16 z;
#pragma unroll
    for (int i = 0; i < 16; ++i) z[i] = 0.f;
    for (int it = 0; it < 32; ++it) {
      const char* tl = kvg + (size_t)((it >> 1) * 4 + (it & 1)) * KV_STRIDE;
      const bf16x8 k0 = *(const bf16x8*)(tl + aK0);
      const bf16x4 k1 = *(const bf16x4*)(tl + aK1);
      const bf16x8 v0 = *(const bf16x8*)(tl + aV);
      const bf16x8 v1 = *(const bf16x8*)(tl + aV + 672);
      f32x16 s = __builtin_amdgcn_mfma_f32_32x32x16_bf16(k0, q0, z, 0, 0, 0);
      s = __builtin_amdgcn_mfma_f32_32x32x8bf16_1k(k1, q1, s, 0, 0, 0);
      float mx = m;
#pragma unroll
      for (int i = 0; i < 16; ++i) mx = fmaxf(mx, s[i]);
      mx = fmaxf(mx, __shfl_xor(mx, 32));
      const float alpha = __builtin_amdgcn_exp2f(m - mx);
      m = mx;
#pragma unroll
      for (int i = 0; i < 16; ++i) { O[i] *= alpha; s[i] -= mx; }
      const bf16x8 p0 = packP(s, 0), p1 = packP(s, 8);
      O = __builtin_amdgcn_mfma_f32_32x32x16_bf16(v0, p0, O, 0, 0, 0);
      O = __builtin_amdgcn_mfma_f32_32x32x16_bf16(v1, p1, O, 0, 0, 0);
    }
  }
  BARRIER();
  float* cb = (float*)smem + qg * (13 * 64);
  if (kh == 1) {
#pragma unroll
    for (int i = 0; i < 12; ++i) cb[i * 64 + lane] = O[i];
    cb[12 * 64 + lane] = m;
  }
  BARRIER();
  if (kh == 0) {
    const float m2 = cb[12 * 64 + lane];
    const float mm = fmaxf(m, m2);
    const float a1 = __builtin_amdgcn_exp2f(m - mm), a2 = __builtin_amdgcn_exp2f(m2 - mm);
    float o[12];
#pragma unroll
    for (int i = 0; i < 12; ++i) o[i] = O[i] * a1 + cb[i * 64 + lane] * a2;
    const float l = __shfl(o[8], lane | 32);
    const float inv = 1.f / l;
    const int token = (qb * 4 + qg) * 32 + r;
    bf16* dst = (bf16*)(ws + WS_O) + (size_t)token * D_;
#pragma unroll
    for (int g = 0; g < 3; ++g) {
      if (g < 2 || h == 0) {
        const int c0 = head * 20 + 8 * g + 4 * h;
        const int u = (c0 & 31) >> 2;
        const int pos = (c0 & ~31) + 8 * (u & 3) + 4 * (u >> 2);
        u32x2 w; w[0] = pack2(o[4 * g] * inv, o[4 * g + 1] * inv); w[1] = pack2(o[4 * g + 2] * inv, o[4 * g + 3] * inv);
        *(u32x2*)(dst + pos) = w;
      }
    }
  }
}

__global__ __launch_bounds__(256, 1) void k3_out(const bf16* __restrict__ x, const bf16* __restrict__ wo,
                                                 const char* __restrict__ ws, bf16* __restrict__ out) {
  extern __shared__ __attribute__((aligned(16))) char smem[];
  const int b = blockIdx.x, tid = threadIdx.x, wave = __builtin_amdgcn_readfirstlane(tid >> 6), lane = tid & 63;
  const int tn = b >> 5, tm = (b & 7) * 4 + ((b >> 3) & 3);
  const int r = lane & 15, g = lane >> 4;
  const int token = tm * 64 + wave * 16 + r;
  const bf16* ap = (const bf16*)(ws + WS_O) + (size_t)token * D_ + 8 * g;
  u32x4 af[20];
#pragma unroll
  for (int s = 0; s < 20; ++s) af[s] = *(const u32x4*)(ap + 32 * s);
#pragma unroll
  for (int c = 0; c < 5; ++c)
#pragma unroll
    for (int k = 0; k < 5; ++k) {
      const int P = c * 20 + wave * 5 + k;
      const int i = P * 64 + lane;
      const int row = i / 10, ch = i - row * 10;
      glds16(wo + (size_t)row * D_ + tn * 80 + ch * 8, smem + P * 1024);
    }
  u32x2 xr[5], nr[5];
#pragma unroll
  for (int j = 0; j < 5; ++j) {
    xr[j] = *(const u32x2*)(x + (size_t)token * D_ + tn * 80 + j * 16 + 4 * g);
    nr[j] = *(const u32x2*)((const bf16*)(ws + WS_XN) + (size_t)token * D_ + tn * 80 + j * 16 + 4 * g);
  }
  const int sel0 = ((const int*)(ws + WS_SEL))[0], sel1 = ((const int*)(ws + WS_SEL))[1];
  const int q = (lane & 15) >> 2, p = lane & 3;
  const int abase = (4 * g + q) * 160 + 8 * p;
  f32x4 acc[5];
#pragma unroll
  for (int j = 0; j < 5; ++j) { acc[j][0] = 0.f; acc[j][1] = 0.f; acc[j][2] = 0.f; acc[j][3] = 0.f; }
#pragma unroll
  for (int c = 0; c < 5; ++c) {
    switch (c) {
      case 0: WAITVM(20); break; case 1: WAITVM(15); break; case 2: WAITVM(10); break; case 3: WAITVM(5); break;
      default: WAITVM(0); break;
    }
    BARRIER();
#pragma unroll
    for (int ks = 0; ks < 4; ++ks) {
      const int s = c * 4 + ks;
      const bf16x8 ab = __builtin_bit_cast(bf16x8, af[s]);
#pragma unroll
      for (int j = 0; j < 5; ++j) {
        s16x4 w0 = trread(smem + (32 * s) * 160 + j * 32 + abase);
        s16x4 w1 = trread(smem + (32 * s + 16) * 160 + j * 32 + abase);
        acc[j] = __builtin_amdgcn_mfma_f32_16x16x32_bf16(comb(w0, w1), ab, acc[j], 0, 0, 0);
      }
    }
  }
  const bool is0 = token == sel0, is1 = token == sel1;
#pragma unroll
  for (int j = 0; j < 5; ++j) {
    const int n = tn * 80 + j * 16 + 4 * g;
    float xv[4] = {lo16(xr[j][0]), hi16(xr[j][0]), lo16(xr[j][1]), hi16(xr[j][1])};
    float nv[4] = {lo16(nr[j][0]), hi16(nr[j][0]), lo16(nr[j][1]), hi16(nr[j][1])};
    if (is0 || is1) {
      const int t = is0 ? 0 : 1;
      const float sig = ((const float*)(ws + WS_SEL))[2 + t];
      const float* yp = (const float*)(ws + WS_YP) + t * 640 + n;
      f32x4 y = {0.f, 0.f, 0.f, 0.f};
      for (int k = 0; k < N_MOE; ++k) { f32x4 v = *(const f32x4*)(yp + (size_t)k * 1280); y += v; }
#pragma unroll
      for (int i = 0; i < 4; ++i) nv[i] = rbf(nv[i] + rbf(rbf(y[i]) * sig));
    }
    float o[4];
#pragma unroll
    for (int i = 0; i < 4; ++i) o[i] = rbf(xv[i] + rbf(acc[j][i])) + nv[i];
    u32x2 w; w[0] = pack2(o[0], o[1]); w[1] = pack2(o[2], o[3]);
    *(u32x2*)(out + (size_t)token * D_ + n) = w;
  }
}

extern "C" void kernel_launch(void* const* d_in, const int* in_sizes, int n_in, void* d_out, int out_size, void* d_ws,
                              size_t ws_size, hipStream_t stream) {
  const bf16* x = (const bf16*)d_in[0];
  const bf16* wqkv = (const bf16*)d_in[1];
  const bf16* wo = (const bf16*)d_in[2];
  const bf16* wr = (const bf16*)d_in[4];
  const bf16* wg = (const bf16*)d_in[5];
  const bf16* wu = (const bf16*)d_in[6];
  const bf16* wd = (const bf16*)d_in[7];
  char* ws = (char*)d_ws;
  constexpr int LDS1 = 131072 + 1536, LDS2 = 3 * SLOT + 4096, LDS3B = 102400;
  (void)hipFuncSetAttribute((const void*)k1_ln_qkv, hipFuncAttributeMaxDynamicSharedMemorySize, LDS1);
  (void)hipFuncSetAttribute((const void*)k3_out, hipFuncAttributeMaxDynamicSharedMemorySize, LDS3B);
  k1_ln_qkv<<<256, 256, LDS1, stream>>>(x, wqkv, wr, ws);
  k2_attn<<<N_MOE_PAD + 512, 512, LDS2, stream>>>(wg, wu, wd, ws);
  k3_out<<<256, 256, LDS3B, stream>>>(x, wo, ws, (bf16*)d_out);
}
```

```cpp
#include <hip/hip_runtime.h>
#include <hip/hip_bf16.h>

typedef __bf16 bf16;
typedef __bf16 bf16x2 __attribute__((ext_vector_type(2)));
typedef __bf16 bf16x4 __attribute__((ext_vector_type(4)));
typedef __bf16 bf16x8 __attribute__((ext_vector_type(8)));
typedef short s16x4 __attribute__((ext_vector_type(4)));
typedef float f32x4 __attribute__((ext_vector_type(4)));
typedef float f32x16 __attribute__((ext_vector_type(16)));
typedef unsigned u32x2 __attribute__((ext_vector_type(2)));
typedef unsigned u32x4 __attribute__((ext_vector_type(4)));
#define LDS3 __attribute__((address_space(3)))

constexpr int S_ = 2048, D_ = 640, H_ = 32, F_ = 1728;
constexpr int QT_STRIDE = 1280;
constexpr int KV_STRIDE = 2624;
constexpr size_t WS_Q = 0, WS_KV = 0x300000, WS_O = 0x900000, WS_XX = 0xC00000, WS_R = 0x1200000,
                 WS_SEL = 0x1204000, WS_YP = 0x1210000, WS_WO = 0x1300000;
constexpr int N_MOE = 108;
constexpr float QSCALE = 0.22360679774997896f * 1.4426950408889634f;

__device__ __forceinline__ float bf2f(bf16 v) { return (float)v; }
__device__ __forceinline__ float rbf(float v) { return (float)(bf16)v; }
__device__ __forceinline__ unsigned pack2(float a, float b) {
  bf16x2 p; p[0] = (bf16)a; p[1] = (bf16)b; return __builtin_bit_cast(unsigned, p);
}
__device__ __forceinline__ float lo16(unsigned u) { return __builtin_bit_cast(float, u << 16); }
__device__ __forceinline__ float hi16(unsigned u) { return __builtin_bit_cast(float, u & 0xffff0000u); }
__device__ __forceinline__ void glds16(const void* g, void* l) {
  __builtin_amdgcn_global_load_lds((const unsigned*)g, (LDS3 unsigned*)l, 16, 0, 0);
}
__device__ __forceinline__ s16x4 trread(const char* l) {
  return __builtin_amdgcn_ds_read_tr16_b64_v4i16((LDS3 s16x4*)l);
}
__device__ __forceinline__ bf16x8 comb(s16x4 a, s16x4 b) {
  typedef short s16x8 __attribute__((ext_vector_type(8)));
  s16x8 r = __builtin_shufflevector(a, b, 0, 1, 2, 3, 4, 5, 6, 7);
  return __builtin_bit_cast(bf16x8, r);
}
__device__ __forceinline__ float dot2(unsigned a, unsigned b, float c) {
  return __builtin_amdgcn_fdot2_f32_bf16(__builtin_bit_cast(bf16x2, a), __builtin_bit_cast(bf16x2, b), c, false);
}
#define TS(k)
#define TSDECL
#define TSARG
#define TSPASS
#define TSOUT(kern)
#define WAITVM(n) asm volatile("s_waitcnt vmcnt(" #n ")" ::: "memory")
template <int V> struct IntC { static constexpr int value = V; };
#define BARRIER() do { asm volatile("s_waitcnt lgkmcnt(0)" ::: "memory"); __builtin_amdgcn_s_barrier(); } while (0)

__device__ __forceinline__ float red32(float v) {
  v += __builtin_bit_cast(float, __builtin_amdgcn_update_dpp(0, __builtin_bit_cast(int, v), 0x128, 0xf, 0xf, false));
  v += __builtin_bit_cast(float, __builtin_amdgcn_update_dpp(0, __builtin_bit_cast(int, v), 0x124, 0xf, 0xf, false));
  v += __builtin_bit_cast(float, __builtin_amdgcn_update_dpp(0, __builtin_bit_cast(int, v), 0x122, 0xf, 0xf, false));
  v += __builtin_bit_cast(float, __builtin_amdgcn_update_dpp(0, __builtin_bit_cast(int, v), 0x121, 0xf, 0xf, false));
  return v + __shfl_xor(v, 16);
}
__device__ __forceinline__ void k1_piece(const bf16* __restrict__ x, const bf16* __restrict__ w, char* smem, int tm, int tn,
                                         int wave, int lane, int c, int i, int kind) {
  char* sl = smem + (c & 3) * 32768;
  const int P = wave * 4 + i;
  if (kind == 0) {
    const int row = 8 * P + (lane >> 3);
    const int ch = (lane & 7) ^ ((row >> 1) & 7);
    glds16(x + (size_t)(tm * 128 + row) * D_ + 64 * c + ch * 8, sl + P * 1024);
  } else {
    const int lrow = lane >> 4, row = 4 * P + lrow;
    const int ch = (lane & 15) ^ ((lrow << 2) | (P & 3));
    glds16(w + (size_t)(64 * c + row) * 1920 + tn * 128 + ch * 8, sl + 16384 + P * 1024);
  }
}

template <bool SWAP>
__device__ __forceinline__ void k1_gemm(const bf16* __restrict__ x, const bf16* __restrict__ w, char* __restrict__ ws,
                                        char* smem, int tm, int tn TSARG) {
  const int tid = threadIdx.x, wave = __builtin_amdgcn_readfirstlane(tid >> 6), lane = tid & 63, r = lane & 31, h = lane >> 5;
  const int g = lane >> 4, q = (lane & 15) >> 2, p = lane & 3;
  int abase[4][2], xoff[4];
#pragma unroll
  for (int j = 0; j < 4; ++j) {
#pragma unroll
    for (int rd = 0; rd < 2; ++rd) {
      int cb = (j + wave) & 3;
      int ch = cb * 4 + (g & 1) * 2 + (p >> 1);
      int swz = (q << 2) | ((2 * h + rd) & 3);
      abase[j][rd] = 16384 + 256 * (8 * h + 4 * rd + q) + 16 * (ch ^ swz) + 8 * (p & 1);
    }
    xoff[j] = (wave * 32 + r) * 128 + (((2 * j + h) ^ ((r >> 1) & 7)) << 4);
  }
  f32x16 acc[4];
#pragma unroll
  for (int j = 0; j < 4; ++j)
#pragma unroll
    for (int i = 0; i < 16; ++i) acc[j][i] = 0.f;
  float sq = 0.f, sq2 = 0.f;
  f32x4 sm4 = {0.f, 0.f, 0.f, 0.f}, cs4 = {0.f, 0.f, 0.f, 0.f};
  const unsigned ONES = 0x3f803f80u;
  const s16x4 one4 = {0x3f80, 0x3f80, 0x3f80, 0x3f80};
  bf16x8 wf[2][4];
  u32x4 xf[2];
  auto loadw = [&](const char* sl, int ph, bf16x8 (&dst)[4], u32x4& xd) {
    xd = *(const u32x4*)(sl + xoff[ph]);
#pragma unroll
    for (int j = 0; j < 4; ++j) dst[j] = comb(trread(sl + ph * 4096 + abase[j][0]), trread(sl + ph * 4096 + abase[j][1]));
  };
  WAITVM(16);
  TS(3);
  BARRIER();
  TS(4);
  k1_piece(x, w, smem, tm, tn, wave, lane, 3, 0, 0);
  k1_piece(x, w, smem, tm, tn, wave, lane, 3, 0, 1);
  loadw(smem, 0, wf[0], xf[0]);
  auto chunk = [&](int c, auto mode) {
    constexpr int MODE = decltype(mode)::value;
    const char* sl = smem + (c & 3) * 32768;
    const char* sx = smem + ((c + 1) & 3) * 32768;
#pragma unroll
    for (int ph = 0; ph < 4; ++ph) {
      const u32x4 xa = xf[ph & 1];
      const bf16x8 xb = __builtin_bit_cast(bf16x8, xa);
      const u32x4 wu = __builtin_bit_cast(u32x4, wf[ph & 1][0]);
      const int cn = ph == 3 ? c + 4 : c + 3;
      const bool refill = MODE == 0 || (MODE == 1 && ph < 3);
#pragma unroll
      for (int j = 0; j < 4; ++j) {
        if (SWAP) acc[j] = __builtin_amdgcn_mfma_f32_32x32x16_bf16(wf[ph & 1][j], xb, acc[j], 0, 0, 0);
        else      acc[j] = __builtin_amdgcn_mfma_f32_32x32x16_bf16(xb, wf[ph & 1][j], acc[j], 0, 0, 0);
        if (j == 0) {
          __builtin_amdgcn_sched_barrier(0);
          if (ph == 3) {
            if (MODE < 2) WAITVM(16); else WAITVM(0);
            BARRIER();
          }
          loadw(ph == 3 ? sx : sl, (ph + 1) & 3, wf[(ph + 1) & 1], xf[(ph + 1) & 1]);
          __builtin_amdgcn_sched_barrier(0);
        }
        if ((j & 1) == 1 && refill) k1_piece(x, w, smem, tm, tn, wave, lane, cn, (ph + 1) & 3, j >> 1);
        const float lo = lo16(xa[j]), hi = hi16(xa[j]);
        sq = __builtin_fmaf(lo, lo, sq); sq2 = __builtin_fmaf(hi, hi, sq2);
        if (j & 1) {
          u32x2 xs = {xa[j - 1], xa[j]}, wsum = {wu[j - 1], wu[j]};
          sm4 = __builtin_amdgcn_mfma_f32_4x4x4bf16_1k(one4, __builtin_bit_cast(s16x4, xs), sm4, 0, 0, 0);
          cs4 = __builtin_amdgcn_mfma_f32_4x4x4bf16_1k(one4, __builtin_bit_cast(s16x4, wsum), cs4, 0, 0, 0);
        }
      }
      __builtin_amdgcn_sched_barrier(0);
    }
  };
#pragma unroll 1
  for (int c = 0; c < 6; ++c) chunk(c, IntC<0>{});
  chunk(6, IntC<1>{});
#pragma unroll 1
  for (int c = 7; c < 10; ++c) chunk(c, IntC<2>{});
  float sm = sm4[0], cs = cs4[0];
  sq += sq2;
  TS(5);
  sm += __shfl_xor(sm, 32); sq += __shfl_xor(sq, 32); cs += __shfl_xor(cs, 32);
  float* s_mean = (float*)(smem + 131072);
  float* s_rstd = s_mean + 128;
  float* s_cs = s_rstd + 128;
  const float mean = sm * (1.f / 640.f);
  const float var = fmaxf(sq * (1.f / 640.f) - mean * mean, 0.f);
  const float meanb = rbf(mean);
  const float rstd = 1.f / rbf(sqrtf(rbf(rbf(var) + 1e-5f)));
  if (h == 0) { s_mean[wave * 32 + r] = meanb; s_rstd[wave * 32 + r] = rstd; s_cs[wave * 32 + r] = cs; }
  unsigned* s_tab = (unsigned*)(s_cs + 128);
  const int part = tn / 5;
  const int stride = part == 0 ? QT_STRIDE : KV_STRIDE;
  if (SWAP && tid < 32) {
    const int nn = (tn - part * 5) * 128 + tid * 4, head = nn / 20, d0 = nn - head * 20;
    s_tab[tid] = (unsigned)(head * 64 * stride) + (d0 < 16 ? (d0 >> 3) * 512 + (d0 & 4) * 2 : 0x80000400u);
  }
  BARRIER();
  const int tile = tm * 4 + wave;
  if (SWAP) {
    const float sc = part == 0 ? rstd * QSCALE : rstd;
    char* base = ws + (part == 0 ? WS_Q : WS_KV) + (size_t)tile * stride;
    const float msc = -meanb * sc;
#pragma unroll
    for (int j = 0; j < 4; ++j) {
      const int cb = (j + wave) & 3;
#pragma unroll
      for (int g4 = 0; g4 < 4; ++g4) {
        const int nl = cb * 32 + 8 * g4 + 4 * h;
        const f32x4 c4 = *(const f32x4*)(s_cs + nl);
        const unsigned tb = s_tab[nl >> 2];
        float v0 = sc * acc[j][4 * g4 + 0] + msc * c4[0], v1 = sc * acc[j][4 * g4 + 1] + msc * c4[1];
        float v2 = sc * acc[j][4 * g4 + 2] + msc * c4[2], v3 = sc * acc[j][4 * g4 + 3] + msc * c4[3];
        u32x2 o; o[0] = pack2(v0, v1); o[1] = pack2(v2, v3);
        const unsigned off = (tb & 0x7fffffffu) + ((int)tb < 0 ? r * 8 : r * 16);
        *(u32x2*)(base + off) = o;
      }
    }
  } else {
#pragma unroll
    for (int j = 0; j < 4; ++j) {
      const int cb = (j + wave) & 3;
      const int nl = cb * 32 + r;
      const int nn = (tn - 10) * 128 + nl;
      const int head = nn / 20, d = nn - head * 20;
      const float c1 = s_cs[nl];
      char* dst = ws + WS_KV + (size_t)(head * 64 + tile) * KV_STRIDE + 1280 + h * 336 + d * 16;
#pragma unroll
      for (int s = 0; s < 2; ++s) {
        float v[8];
#pragma unroll
        for (int hh = 0; hh < 2; ++hh) {
          const int t0 = wave * 32 + 16 * s + 8 * hh + 4 * h;
          const f32x4 m4 = *(const f32x4*)(s_mean + t0), r4 = *(const f32x4*)(s_rstd + t0);
#pragma unroll
          for (int i = 0; i < 4; ++i) v[hh * 4 + i] = r4[i] * (acc[j][8 * s + 4 * hh + i] - m4[i] * c1);
        }
        u32x4 o; o[0] = pack2(v[0], v[1]); o[1] = pack2(v[2], v[3]); o[2] = pack2(v[4], v[5]); o[3] = pack2(v[6], v[7]);
        *(u32x4*)(dst + s * 672) = o;
        if (d == 19) { u32x4 one = {ONES, ONES, ONES, ONES}; *(u32x4*)(dst + s * 672 + 16) = one; }
      }
    }
  }
}

__global__ __launch_bounds__(256, 2) void k1_ln_qkv(const bf16* __restrict__ x, const bf16* __restrict__ w,
                                                    const bf16* __restrict__ wr, const bf16* __restrict__ wo,
                                                    char* __restrict__ ws) {
  extern __shared__ __attribute__((aligned(16))) char smem[];
  const int b = blockIdx.x, tid = threadIdx.x, wave = __builtin_amdgcn_readfirstlane(tid >> 6), lane = tid & 63, r = lane & 31, h = lane >> 5;
  TSDECL; TS(0);
  const bool gemm = b < 240;
  const int xcd = b & 7, i = (xcd & 1) * 30 + (b >> 3);
  const int tn = i >> 2, tm = (xcd >> 1) * 4 + (i & 3);
  const int lrow = b * 8 + wave * 2 + h;
  u32x2 xr[5], wv[5];
#pragma unroll
  for (int k = 0; k < 5; ++k) {
    xr[k] = *(const u32x2*)(x + (size_t)lrow * D_ + r * 20 + k * 4);
    wv[k] = *(const u32x2*)(wr + r * 20 + k * 4);
  }
  if (gemm) {
#pragma unroll
    for (int i = 0; i < 8; ++i) k1_piece(x, w, smem, tm, tn, wave, lane, 0, i & 3, i >> 2);
#pragma unroll
    for (int c = 1; c < 3; ++c)
#pragma unroll
      for (int k = 0; k < 8; ++k) k1_piece(x, w, smem, tm, tn, wave, lane, c, k & 3, k >> 2);
  } else {
    const int k0 = (b - 240) * 40;
#pragma unroll
    for (int j = 0; j < 13; ++j) {
      const int idx = tid + 256 * j;
      if (idx < 3200) {
        const int row = idx / 80, ch = idx - row * 80, t8 = ch / 10, cc = ch - t8 * 10;
        *(u32x4*)(ws + WS_WO + (size_t)t8 * 102400 + (k0 + row) * 160 + cc * 16) = *(const u32x4*)(wo + (size_t)(k0 + row) * D_ + ch * 8);
      }
    }
  }
  TS(1);
  u32x2 o2[5];
  float rr = 0.f;
  {
    float v[20];
#pragma unroll
    for (int k = 0; k < 5; ++k) {
      v[4 * k] = lo16(xr[k][0]); v[4 * k + 1] = hi16(xr[k][0]); v[4 * k + 2] = lo16(xr[k][1]); v[4 * k + 3] = hi16(xr[k][1]);
    }
    float s = 0.f;
#pragma unroll
    for (int k = 0; k < 20; ++k) s += v[k];
    const float mean = red32(s) * (1.f / 640.f);
    float q = 0.f;
#pragma unroll
    for (int k = 0; k < 20; ++k) { float d = v[k] - mean; q += d * d; }
    q = red32(q);
    const float meanb = rbf(mean);
    const float den = rbf(sqrtf(rbf(rbf(q * (1.f / 640.f)) + 1e-5f)));
    const float rc = 1.f / den;
#pragma unroll
    for (int k = 0; k < 20; ++k) {
      const float d = rbf(v[k] - meanb), q0 = d * rc;
      v[k] = rbf(__builtin_fmaf(__builtin_fmaf(-q0, den, d), rc, q0));
    }
#pragma unroll
    for (int k = 0; k < 5; ++k) {
      rr += v[4 * k] * lo16(wv[k][0]) + v[4 * k + 1] * hi16(wv[k][0]) + v[4 * k + 2] * lo16(wv[k][1]) + v[4 * k + 3] * hi16(wv[k][1]);
      o2[k][0] = pack2(v[4 * k], v[4 * k + 1]); o2[k][1] = pack2(v[4 * k + 2], v[4 * k + 3]);
    }
    rr = red32(rr);
  }
  TS(2);
  if (gemm) {
    if (tn < 10) k1_gemm<true>(x, w, ws, smem, tm, tn TSPASS);
    else k1_gemm<false>(x, w, ws, smem, tm, tn TSPASS);
  }
#pragma unroll
  for (int k = 0; k < 5; ++k) {
    const int cg = 5 * r + k, t8 = cg / 20, rem = cg - t8 * 20;
    u32x4 o = {xr[k][0], xr[k][1], o2[k][0], o2[k][1]};
    *(u32x4*)(ws + WS_XX + (size_t)(((lrow >> 4) * 8 + t8) * 5 + (rem >> 2)) * 1024 + ((rem & 3) * 16 + (lrow & 15)) * 16) = o;
  }
  if (r == 0) ((float*)(ws + WS_R))[lrow] = rbf(rr);
  TS(6);
  TSOUT(0);
}

constexpr int SLOT = 12288;
constexpr float BIAS = 88.f;
constexpr float RECENTER = 1.0f;

__device__ __forceinline__ bf16x8 packP(const f32x16& s, int o) {
  bf16x8 p;
#pragma unroll
  for (int i = 0; i < 8; ++i) p[i] = (bf16)__builtin_amdgcn_exp2f(s[o + i]);
  return p;
}

__device__ __forceinline__ void moe_block(int mb, const bf16* __restrict__ wg, const bf16* __restrict__ wu,
                                          const bf16* __restrict__ wd, char* __restrict__ ws, char* smem) {
  const int tid = threadIdx.x, wave = tid >> 6, lane = tid & 63;
  const float* rv = (const float*)(ws + WS_R);
  unsigned long long* s_key = (unsigned long long*)smem;
  float* s_xn = (float*)(smem + 256);
  float* s_red = (float*)(smem + 256 + 5120);
  float* s_h = (float*)(smem + 256 + 5120 + 15360);
  const int f0 = mb * 16, quad = tid & 1, mat = (tid >> 1) & 1, rg = tid >> 2;
  const bf16* wp = (mat ? wu : wg) + f0 + quad * 8;
  float rl[8];
#pragma unroll
  for (int i = 0; i < 8; ++i) rl[i] = rv[tid + 256 * i];
  u32x4 wl[10];
#pragma unroll
  for (int i = 0; i < 10; ++i) wl[i] = *(const u32x4*)(wp + (size_t)(rg + 64 * i) * F_);
  unsigned long long m1 = 0, m2 = 0;
#pragma unroll
  for (int i = 0; i < 8; ++i) {
    unsigned u = __builtin_bit_cast(unsigned, rl[i]);
    u = (u & 0x80000000u) ? ~u : (u | 0x80000000u);
    unsigned long long k = ((unsigned long long)u << 32) | (unsigned)(0xFFFFFFFFu - (tid + 256 * i));
    m2 = k > m1 ? m1 : (k > m2 ? k : m2);
    m1 = k > m1 ? k : m1;
  }
#pragma unroll
  for (int o = 32; o >= 1; o >>= 1) {
    unsigned long long t1 = __shfl_xor(m1, o), t2 = __shfl_xor(m2, o);
    unsigned long long lo = t1 > m1 ? m1 : t1, hi2 = t2 > m2 ? t2 : m2;
    m1 = t1 > m1 ? t1 : m1;
    m2 = lo > hi2 ? lo : hi2;
  }
  if (lane == 0) { s_key[wave * 2] = m1; s_key[wave * 2 + 1] = m2; }
  __syncthreads();
  m1 = 0; m2 = 0;
#pragma unroll
  for (int i = 0; i < 8; ++i) {
    unsigned long long k = s_key[i];
    m2 = k > m1 ? m1 : (k > m2 ? k : m2);
    m1 = k > m1 ? k : m1;
  }
  const int i0 = (int)(0xFFFFFFFFu - (unsigned)m1), i1 = (int)(0xFFFFFFFFu - (unsigned)m2);
  if (mb == 0 && tid < 2) {
    int id = tid == 0 ? i0 : i1;
    ((int*)(ws + WS_SEL))[tid] = id;
    ((float*)(ws + WS_SEL))[2 + tid] = rbf(1.f / (1.f + __expf(-rv[id])));
  }
  for (int i = tid; i < 320; i += 256) {
    const int t = i >= 160, cg = i - t * 160, tok = t ? i1 : i0, t8 = cg / 20, rem = cg - t8 * 20;
    const u32x2 v = *(const u32x2*)(ws + WS_XX + (size_t)(((tok >> 4) * 8 + t8) * 5 + (rem >> 2)) * 1024 + ((rem & 3) * 16 + (tok & 15)) * 16 + 8);
    f32x4 f = {lo16(v[0]), hi16(v[0]), lo16(v[1]), hi16(v[1])};
    *(f32x4*)(s_xn + t * 640 + cg * 4) = f;
  }
  __syncthreads();
  float a[2][8];
#pragma unroll
  for (int j = 0; j < 8; ++j) { a[0][j] = 0.f; a[1][j] = 0.f; }
#pragma unroll
  for (int i = 0; i < 10; ++i) {
    float x0 = s_xn[rg + 64 * i], x1 = s_xn[640 + rg + 64 * i];
    asm volatile("" : "+v"(wl[i]));
#pragma unroll
    for (int j = 0; j < 4; ++j) {
      float wl0 = lo16(wl[i][j]), wh0 = hi16(wl[i][j]);
      a[0][2 * j] += x0 * wl0; a[0][2 * j + 1] += x0 * wh0; a[1][2 * j] += x1 * wl0; a[1][2 * j + 1] += x1 * wh0;
    }
  }
  const int c = tid % 80, rgd = tid / 80;
  __builtin_amdgcn_sched_barrier(0);
  asm volatile("" ::: "memory");
  u32x4 dl[6];
#pragma unroll
  for (int i = 0; i < 6; ++i) {
    int f = rgd + 3 * i;
    dl[i] = *(const u32x4*)(wd + (size_t)(f0 + (f < 16 ? f : 15)) * D_ + c * 8);
  }
#pragma unroll
  for (int t = 0; t < 2; ++t)
#pragma unroll
    for (int j = 0; j < 8; ++j) {
      float v = a[t][j];
      v += __shfl_xor(v, 4); v += __shfl_xor(v, 8); v += __shfl_xor(v, 16); v += __shfl_xor(v, 32);
      if (lane < 4) s_red[(wave * 4 + lane) * 16 + t * 8 + j] = v;
    }
  __syncthreads();
  if (tid < 32) {
    int t = tid >> 4, col = tid & 15, qd = col >> 3, j = col & 7;
    float gs = 0.f, us = 0.f;
#pragma unroll
    for (int w = 0; w < 4; ++w) { gs += s_red[(w * 4 + qd) * 16 + t * 8 + j]; us += s_red[(w * 4 + 2 + qd) * 16 + t * 8 + j]; }
    gs = rbf(gs); us = rbf(us);
    float sl = rbf(gs / (1.f + __expf(-gs)));
    s_h[t * 16 + col] = rbf(sl * us);
  }
  __syncthreads();
#pragma unroll
  for (int j = 0; j < 8; ++j) { a[0][j] = 0.f; a[1][j] = 0.f; }
#pragma unroll
  for (int i = 0; i < 6; ++i) {
    int f = rgd + 3 * i;
    float h0 = (rgd < 3 && f < 16) ? s_h[f] : 0.f, h1 = (rgd < 3 && f < 16) ? s_h[16 + f] : 0.f;
    asm volatile("" : "+v"(dl[i]));
#pragma unroll
    for (int j = 0; j < 4; ++j) {
      float wl0 = lo16(dl[i][j]), wh0 = hi16(dl[i][j]);
      a[0][2 * j] += h0 * wl0; a[0][2 * j + 1] += h0 * wh0; a[1][2 * j] += h1 * wl0; a[1][2 * j + 1] += h1 * wh0;
    }
  }
  if (rgd < 3) {
#pragma unroll
    for (int t = 0; t < 2; ++t)
#pragma unroll
      for (int j = 0; j < 8; ++j) s_red[(rgd * 2 + t) * 640 + c * 8 + j] = a[t][j];
  }
  __syncthreads();
  float* yp = (float*)(ws + WS_YP) + (size_t)mb * 1280;
  for (int i = tid; i < 1280; i += 256) yp[i] = s_red[i] + s_red[1280 + i] + s_red[2560 + i];
}

__global__ __launch_bounds__(256, 3) void k2_attn(const bf16* __restrict__ wg, const bf16* __restrict__ wu,
                                                  const bf16* __restrict__ wd, char* __restrict__ ws) {
  extern __shared__ __attribute__((aligned(16))) char smem[];
  TSDECL; TS(0);
  if (blockIdx.x >= 512) {
    moe_block(blockIdx.x - 512, wg, wu, wd, ws, smem);
    TS(7);
    TSOUT(1);
    return;
  }
  const int a = blockIdx.x;
  const int head = (a & 7) * 4 + ((a >> 3) & 3), qb = a >> 5;
  const int tid = threadIdx.x, wave = __builtin_amdgcn_readfirstlane(tid >> 6), lane = tid & 63, r = lane & 31, h = lane >> 5;
  const char* kvg = ws + WS_KV + (size_t)head * 64 * KV_STRIDE;
  auto issue = [&](int c) {
    const char* src = kvg + (size_t)c * (4 * KV_STRIDE) + wave * 1024 + lane * 16;
    char* dst = smem + (c & 3) * SLOT + wave * 1024;
#pragma unroll
    for (int i = 0; i < 3; ++i) glds16(src + i * 4096, dst + i * 4096);
  };
  const char* qp = ws + WS_Q + (size_t)(head * 64 + qb * 4 + wave) * QT_STRIDE;
  const bf16x8 q0 = *(const bf16x8*)(qp + lane * 16);
  u32x2 q1u = *(const u32x2*)(qp + 1024 + r * 8);
  issue(0);
  issue(1);
  issue(2);
  if (h) { q1u[0] = 0; q1u[1] = 0; }
  const bf16x4 q1 = __builtin_bit_cast(bf16x4, q1u);
  const int aK0 = lane * 16;
  const int aK1 = 1024 + r * 8;
  const int aV = 1280 + h * 336 + (r < 20 ? r : 20) * 16;
  f32x16 O, negm;
#pragma unroll
  for (int i = 0; i < 16; ++i) O[i] = 0.f;
  bool bad = false;
  WAITVM(6);
  BARRIER();
  TS(1);
  {
    f32x16 z;
#pragma unroll
    for (int i = 0; i < 16; ++i) z[i] = 0.f;
    float mx = -3.0e38f;
#pragma unroll
    for (int t = 0; t < 2; ++t) {
      const bf16x8 k0 = *(const bf16x8*)(smem + t * KV_STRIDE + aK0);
      const bf16x4 k1 = *(const bf16x4*)(smem + t * KV_STRIDE + aK1);
      f32x16 s = __builtin_amdgcn_mfma_f32_32x32x16_bf16(k0, q0, z, 0, 0, 0);
      s = __builtin_amdgcn_mfma_f32_32x32x8bf16_1k(k1, q1, s, 0, 0, 0);
#pragma unroll
      for (int i = 0; i < 16; ++i) mx = fmaxf(mx, s[i]);
    }
    mx = fmaxf(mx, __shfl_xor(mx, 32));
#pragma unroll
    for (int i = 0; i < 16; ++i) negm[i] = -mx - BIAS;
    asm volatile("" : "+v"(negm));
  }
  f32x16 s = __builtin_amdgcn_mfma_f32_32x32x16_bf16(*(const bf16x8*)(smem + aK0), q0, negm, 0, 0, 0);
  s = __builtin_amdgcn_mfma_f32_32x32x8bf16_1k(*(const bf16x4*)(smem + aK1), q1, s, 0, 0, 0);
  bf16x8 kA0 = *(const bf16x8*)(smem + KV_STRIDE + aK0);
  bf16x4 kA1 = *(const bf16x4*)(smem + KV_STRIDE + aK1);
  bf16x8 v0 = *(const bf16x8*)(smem + aV);
  bf16x8 v1 = *(const bf16x8*)(smem + aV + 672);
  TS(2);
#pragma unroll 1
  for (int c = 0; c < 16; ++c) {
    const char* sl = smem + (c & 3) * SLOT;
    const char* sx = smem + ((c + 1) & 3) * SLOT;
#pragma unroll
    for (int t = 0; t < 4; ++t) {
      if (t == 2) {
        if (c < 15) {
          if (c < 14) WAITVM(3); else WAITVM(0);
          BARRIER();
          if (c < 13) issue(c + 3);
        }
        if (__builtin_amdgcn_ballot_w64(!(O[8] <= RECENTER)) != 0) {
          float l = __shfl(O[8], lane | 32);
          int e = __builtin_amdgcn_frexp_expf(l) + (int)BIAS;
          float fe = (float)e;
#pragma unroll
          for (int i = 0; i < 16; ++i) { O[i] = __builtin_amdgcn_ldexpf(O[i], -e); negm[i] -= fe; s[i] -= fe; }
        }
      }
      const char* tk = (t < 2 ? sl : sx) + ((t + 2) & 3) * KV_STRIDE;
      const char* tv = (t < 3 ? sl : sx) + ((t + 1) & 3) * KV_STRIDE;
      const bf16x8 kB0 = *(const bf16x8*)(tk + aK0);
      const bf16x4 kB1 = *(const bf16x4*)(tk + aK1);
      const bf16x8 n0 = *(const bf16x8*)(tv + aV);
      const bf16x8 n1 = *(const bf16x8*)(tv + aV + 672);
      __builtin_amdgcn_sched_barrier(0);
      f32x16 sn = __builtin_amdgcn_mfma_f32_32x32x16_bf16(kA0, q0, negm, 0, 0, 0);
      __builtin_amdgcn_sched_barrier(0);
      f32x4 e0;
#pragma unroll
      for (int i = 0; i < 4; ++i) e0[i] = __builtin_amdgcn_exp2f(s[i]);
      __builtin_amdgcn_sched_barrier(0);
      sn = __builtin_amdgcn_mfma_f32_32x32x8bf16_1k(kA1, q1, sn, 0, 0, 0);
      __builtin_amdgcn_sched_barrier(0);
      bf16x8 p0;
#pragma unroll
      for (int i = 0; i < 4; ++i) p0[i] = (bf16)e0[i];
#pragma unroll
      for (int i = 4; i < 8; ++i) p0[i] = (bf16)__builtin_amdgcn_exp2f(s[i]);
      __builtin_amdgcn_sched_barrier(0);
      O = __builtin_amdgcn_mfma_f32_32x32x16_bf16(v0, p0, O, 0, 0, 0);
      __builtin_amdgcn_sched_barrier(0);
      const bf16x8 p1 = packP(s, 8);
      __builtin_amdgcn_sched_barrier(0);
      O = __builtin_amdgcn_mfma_f32_32x32x16_bf16(v1, p1, O, 0, 0, 0);
      __builtin_amdgcn_sched_barrier(0);
      s = sn; kA0 = kB0; kA1 = kB1; v0 = n0; v1 = n1;
    }
  }
  TS(4);
#pragma unroll
  for (int i = 0; i < 12; ++i) bad = bad || !(__builtin_fabsf(O[i]) < 3.0e38f);
  if (__builtin_amdgcn_ballot_w64(bad) != 0) {
    float m = -3.0e38f;
#pragma unroll
    for (int i = 0; i < 16; ++i) O[i] = 0.f;
    f32x16 z;
#pragma unroll
    for (int i = 0; i < 16; ++i) z[i] = 0.f;
#pragma unroll 1
    for (int it = 0; it < 64; ++it) {
      const char* tl = kvg + (size_t)it * KV_STRIDE;
      const bf16x8 k0 = *(const bf16x8*)(tl + aK0);
      const bf16x4 k1 = *(const bf16x4*)(tl + aK1);
      const bf16x8 v0 = *(const bf16x8*)(tl + aV);
      const bf16x8 v1 = *(const bf16x8*)(tl + aV + 672);
      f32x16 s = __builtin_amdgcn_mfma_f32_32x32x16_bf16(k0, q0, z, 0, 0, 0);
      s = __builtin_amdgcn_mfma_f32_32x32x8bf16_1k(k1, q1, s, 0, 0, 0);
      float mx = m;
#pragma unroll
      for (int i = 0; i < 16; ++i) mx = fmaxf(mx, s[i]);
      mx = fmaxf(mx, __shfl_xor(mx, 32));
      const float alpha = __builtin_amdgcn_exp2f(m - mx);
      m = mx;
#pragma unroll
      for (int i = 0; i < 16; ++i) { O[i] *= alpha; s[i] -= mx; }
      const bf16x8 p0 = packP(s, 0), p1 = packP(s, 8);
      O = __builtin_amdgcn_mfma_f32_32x32x16_bf16(v0, p0, O, 0, 0, 0);
      O = __builtin_amdgcn_mfma_f32_32x32x16_bf16(v1, p1, O, 0, 0, 0);
    }
  }
  TS(5);
  {
    const float l = __shfl(O[8], lane | 32);
    const float inv = 1.f / l;
    const int token = (qb * 4 + wave) * 32 + r;
    char* dst = ws + WS_O + (size_t)(token >> 4) * 20480 + (token & 15) * 16;
#pragma unroll
    for (int g = 0; g < 3; ++g) {
      if (g < 2 || h == 0) {
        const int c0 = head * 20 + 8 * g + 4 * h;
        const int u = (c0 & 31) >> 2;
        u32x2 w; w[0] = pack2(O[4 * g] * inv, O[4 * g + 1] * inv); w[1] = pack2(O[4 * g + 2] * inv, O[4 * g + 3] * inv);
        *(u32x2*)(dst + (c0 >> 5) * 1024 + (u & 3) * 256 + (u >> 2) * 8) = w;
      }
    }
  }
  TS(6);
  TSOUT(1);
}

__global__ __launch_bounds__(256, 2) void k3_out(const char* __restrict__ ws_, bf16* __restrict__ out) {
  extern __shared__ __attribute__((aligned(16))) char smem[];
  char* ws = (char*)ws_;
  const int b = blockIdx.x, tid = threadIdx.x, wave = __builtin_amdgcn_readfirstlane(tid >> 6), lane = tid & 63;
  TSDECL; TS(0);
  const int tn = b >> 5, tm = (b & 7) * 4 + ((b >> 3) & 3);
  const int r = lane & 15, g = lane >> 4;
  const int token = tm * 64 + wave * 16 + r;
  u32x4 rs[5];
#pragma unroll
  for (int j = 0; j < 5; ++j)
    rs[j] = *(const u32x4*)(ws + WS_XX + (size_t)(((tm * 4 + wave) * 8 + tn) * 5 + j) * 1024 + lane * 16);
  const char* ap = ws + WS_O + (size_t)(tm * 4 + wave) * 20480 + lane * 16;
  const char* wp = ws + WS_WO + (size_t)tn * 102400 + wave * 5120 + lane * 16;
  auto issue = [&](int c) {
    char* sl = smem + (c & 3) * 36864;
#pragma unroll
    for (int k = 0; k < 5; ++k) glds16(wp + c * 20480 + k * 1024, sl + wave * 5120 + k * 1024);
#pragma unroll
    for (int k = 0; k < 4; ++k) glds16(ap + c * 4096 + k * 1024, sl + 20480 + wave * 4096 + k * 1024);
  };
#pragma unroll 1
  for (int c = 0; c < 4; ++c) issue(c);
  const int sel0 = ((const int*)(ws + WS_SEL))[0], sel1 = ((const int*)(ws + WS_SEL))[1];
  const int q = (lane & 15) >> 2, p = lane & 3;
  const int abase = (4 * g + q) * 160 + 8 * p;
  f32x4 acc[5];
#pragma unroll
  for (int j = 0; j < 5; ++j) { acc[j][0] = 0.f; acc[j][1] = 0.f; acc[j][2] = 0.f; acc[j][3] = 0.f; }
  float* s_y = (float*)(smem + 147456);
  float* s_p = s_y + 160;
#pragma unroll 1
  for (int t = 0; t < 2; ++t) {
    const int sel = t ? sel1 : sel0;
    if ((sel >> 6) == tm) {
      const int cq = tid % 20, pg = tid / 20;
      if (pg < 12) {
        const float* yp = (const float*)(ws + WS_YP) + t * 640 + tn * 80 + cq * 4;
        f32x4 y = {0.f, 0.f, 0.f, 0.f};
#pragma unroll
        for (int k = 0; k < 9; ++k) { int kk = pg + 12 * k; if (kk < N_MOE) y += *(const f32x4*)(yp + (size_t)kk * 1280); }
        *(f32x4*)(s_p + pg * 80 + cq * 4) = y;
      }
      __syncthreads();
      if (tid < 80) {
        float v = 0.f;
#pragma unroll
        for (int k = 0; k < 12; ++k) v += s_p[k * 80 + tid];
        s_y[t * 80 + tid] = rbf(rbf(v) * ((const float*)(ws + WS_SEL))[2 + t]);
      }
      __syncthreads();
    }
  }
  TS(1);
  bf16x8 wf[2][2][5];
  u32x4 af[2][2];
  auto loadw = [&](int st, bf16x8 (&dst)[2][5], u32x4 (&a)[2]) {
    const char* sl = smem + ((st >> 1) & 3) * 36864;
#pragma unroll
    for (int u = 0; u < 2; ++u) {
      const int ph = (st & 1) * 2 + u;
      a[u] = *(const u32x4*)(sl + 20480 + wave * 4096 + ph * 1024 + lane * 16);
#pragma unroll
      for (int j = 0; j < 5; ++j)
        dst[u][j] = comb(trread(sl + (32 * ph) * 160 + j * 32 + abase), trread(sl + (32 * ph + 16) * 160 + j * 32 + abase));
    }
  };
  WAITVM(27);
  TS(2);
  BARRIER();
  TS(3);
  loadw(0, wf[0], af[0]);
#pragma unroll
  for (int st = 0; st < 10; ++st) {
#pragma unroll
    for (int u = 0; u < 2; ++u)
#pragma unroll
      for (int j = 0; j < 5; ++j) {
        acc[j] = __builtin_amdgcn_mfma_f32_16x16x32_bf16(wf[st & 1][u][j], __builtin_bit_cast(bf16x8, af[st & 1][u]), acc[j], 0, 0, 0);
        if (u == 0 && j == 0 && st < 9) {
          __builtin_amdgcn_sched_barrier(0);
          if (st & 1) {
            if (st <= 3) WAITVM(18); else if (st == 5) WAITVM(9); else WAITVM(0);
            BARRIER();
          }
          loadw(st + 1, wf[(st + 1) & 1], af[(st + 1) & 1]);
          if (st == 1) issue(4);
          __builtin_amdgcn_sched_barrier(0);
        }
      }
    __builtin_amdgcn_sched_barrier(0);
  }
  TS(4);
  const bool is0 = token == sel0, is1 = token == sel1;
#pragma unroll
  for (int j = 0; j < 5; ++j) {
    const int n = tn * 80 + j * 16 + 4 * g;
    float xv[4] = {lo16(rs[j][0]), hi16(rs[j][0]), lo16(rs[j][1]), hi16(rs[j][1])};
    float nv[4] = {lo16(rs[j][2]), hi16(rs[j][2]), lo16(rs[j][3]), hi16(rs[j][3])};
    if (is0 || is1) {
      const float* y = s_y + (is0 ? 0 : 80) + j * 16 + 4 * g;
#pragma unroll
      for (int i = 0; i < 4; ++i) nv[i] = rbf(nv[i] + y[i]);
    }
    float o[4];
#pragma unroll
    for (int i = 0; i < 4; ++i) o[i] = rbf(xv[i] + rbf(acc[j][i])) + nv[i];
    u32x2 w; w[0] = pack2(o[0], o[1]); w[1] = pack2(o[2], o[3]);
    *(u32x2*)(out + (size_t)token * D_ + n) = w;
  }
  TS(5);
  TSOUT(2);
}

extern "C" void kernel_launch(void* const* d_in, const int* in_sizes, int n_in, void* d_out, int out_size, void* d_ws,
                              size_t ws_size, hipStream_t stream) {
  const bf16* x = (const bf16*)d_in[0];
  const bf16* wqkv = (const bf16*)d_in[1];
  const bf16* wo = (const bf16*)d_in[2];
  const bf16* wr = (const bf16*)d_in[4];
  const bf16* wg = (const bf16*)d_in[5];
  const bf16* wu = (const bf16*)d_in[6];
  const bf16* wd = (const bf16*)d_in[7];
  char* ws = (char*)d_ws;
  constexpr int LDS1 = 133120, LDS2 = 4 * SLOT, LDS3B = 147456 + 4480;
  (void)hipFuncSetAttribute((const void*)k1_ln_qkv, hipFuncAttributeMaxDynamicSharedMemorySize, LDS1);
  (void)hipFuncSetAttribute((const void*)k3_out, hipFuncAttributeMaxDynamicSharedMemorySize, LDS3B);
  k1_ln_qkv<<<256, 256, LDS1, stream>>>(x, wqkv, wr, wo, ws);
  k2_attn<<<512 + N_MOE, 256, LDS2, stream>>>(wg, wu, wd, ws);
  k3_out<<<256, 256, LDS3B, stream>>>(ws, (bf16*)d_out);
}
```
